```python
import torch

def _softmax(x, axis=-1):
    return torch.softmax(x, dim=axis)

def _gelu(x):
    return torch.nn.functional.gelu(x, approximate='tanh')

def _silu(x):
    return torch.nn.functional.silu(x)

def _sigmoid(x):
    return torch.sigmoid(x)

def _layernorm(x, axis=-1, eps=1e-05):
    m = x.mean(dim=axis, keepdim=True)
    v = x.var(dim=axis, keepdim=True, unbiased=False)
    return (x - m) / torch.sqrt(v + eps)

def _scatter_add(tgt, idx, src, axis=0):
    return tgt.clone().index_add_(axis, idx, src)

def _segment_sum(src, seg, n):
    out = torch.zeros((n,) + src.shape[1:], dtype=src.dtype)
    return out.index_add_(0, seg, src)

def _pad(x, pw):
    flat = tuple((v for pair in reversed(pw) for v in pair))
    return torch.nn.functional.pad(x, flat)
kernel_name = 'standalone__window_attn_twins__vision'

def setup_inputs():

    _w = lambda a: (lambda t: t.to(torch.bfloat16) if t.is_floating_point() else t)(torch.as_tensor(a))
    H = W = 384 // 8 * 8
    C = 96
    return {'x': _w((lambda *s: torch.randn(*s, dtype=torch.bfloat16) * 0.8)(1, H, W, C)), 'wq': _w((lambda *s: torch.randn(*s, dtype=torch.bfloat16) * 0.8)(C, C)), 'wkv': _w((lambda *s: torch.randn(*s, dtype=torch.bfloat16) * 0.8)(C, 2 * C)), 'wo': _w((lambda *s: torch.randn(*s, dtype=torch.bfloat16) * 0.05)(C, C))}

def reference(x, wq, wkv, wo):
    B, H, W, C = x.shape
    win = 8
    nh, nw = (H // win, W // win)
    xs = x.reshape(B, nh, win, nw, win, C).mean(axis=(2, 4)).reshape(B, nh * nw, C)
    q = x.reshape(B, H * W, C) @ wq
    kv = xs @ wkv
    k, v = (kv[..., :C].clone(), kv[..., C:].clone())
    scale = C ** (-0.5)
    p = _softmax(torch.einsum('bqc,bkc->bqk', q, k) * scale, axis=-1)
    o = torch.einsum('bqk,bkc->bqc', p, v) @ wo
    return o.reshape(B, H, W, C)
```

```cpp
#include <hip/hip_runtime.h>
#include <hip/hip_bf16.h>
#include <stdint.h>

typedef __attribute__((ext_vector_type(8))) short bf16x8;
typedef __attribute__((ext_vector_type(16))) float f32x16;
typedef __attribute__((ext_vector_type(4))) unsigned u32x4;
typedef __attribute__((ext_vector_type(2))) float f32x2;
typedef __attribute__((ext_vector_type(2))) __bf16 bf16x2_t;
typedef __attribute__((ext_vector_type(2))) unsigned u32x2;

#define DI __device__ __forceinline__
#define MFMA32(a, b, c) __builtin_amdgcn_mfma_f32_32x32x16_bf16((a), (b), (c), 0, 0, 0)

constexpr int IMG = 384, CH = 96, NPIX = IMG * IMG;
constexpr int NKEY = 2304, NTILE = 36;
constexpr int TILE_BYTES = 24576;
constexpr float KSCALE = 0.10206207261596575f * 1.4426950408889634f;

constexpr size_t WS_KVF = 0;
constexpr size_t WS_KN2 = WS_KVF + (size_t)NTILE * TILE_BYTES;
constexpr size_t WS_VAM = WS_KN2 + NKEY * 4;
constexpr size_t WS_WQF = WS_VAM + NKEY * 4;
constexpr size_t WS_WOF = WS_WQF + 18 * 1024;

DI unsigned cvtpk(float lo, float hi) {
  f32x2 v = {lo, hi};
  bf16x2_t b = __builtin_convertvector(v, bf16x2_t);
  return __builtin_bit_cast(unsigned, b);
}
DI float bf2f(unsigned short u) { return __uint_as_float(((unsigned)u) << 16); }
DI float bfround(float x) { return __uint_as_float(cvtpk(x, 0.f) << 16); }

__global__ __launch_bounds__(256) void prep_kernel(const unsigned short* __restrict__ x, const unsigned short* __restrict__ wq,
                                                   const unsigned short* __restrict__ wkv, const unsigned short* __restrict__ wo,
                                                   char* __restrict__ ws) {
  __shared__ __attribute__((aligned(16))) float part[64 * 96];
  __shared__ float xsf[8 * 96];
  __shared__ float kvf[8 * 192];
  const int tid = threadIdx.x, b = blockIdx.x;
  const int wi = b / 6, jg = b % 6;
#pragma unroll
  for (int k = 0; k < 3; ++k) {
    const int ci = tid + 256 * k, p = ci / 12, c = ci % 12;
    float acc[8];
#pragma unroll
    for (int e = 0; e < 8; ++e) acc[e] = 0.f;
#pragma unroll
    for (int rr = 0; rr < 8; ++rr) {
      const u32x4 v = *reinterpret_cast<const u32x4*>(x + ((size_t)(8 * wi + rr) * IMG + jg * 64 + p) * CH + c * 8);
#pragma unroll
      for (int e = 0; e < 4; ++e) {
        acc[2 * e] += __uint_as_float(v[e] << 16);
        acc[2 * e + 1] += __uint_as_float(v[e] & 0xffff0000u);
      }
    }
#pragma unroll
    for (int e = 0; e < 8; ++e) part[p * 96 + c * 8 + e] = acc[e];
  }
  __syncthreads();
#pragma unroll
  for (int k = 0; k < 3; ++k) {
    const int idx = tid + 256 * k, w = idx / 96, ch = idx % 96;
    float s = 0.f;
#pragma unroll
    for (int pp = 0; pp < 8; ++pp) s += part[(8 * w + pp) * 96 + ch];
    xsf[idx] = bfround(s * (1.0f / 64.0f));
  }
  __syncthreads();
#pragma unroll
  for (int k = 0; k < 6; ++k) {
    const int idx = tid + 256 * k, w = idx / 192, n = idx % 192;
    float s = 0.f;
    for (int c = 0; c < 96; ++c) s = fmaf(xsf[w * 96 + c], bf2f(wkv[c * 192 + n]), s);
    const int key = wi * 48 + jg * 8 + w, t = key >> 6, kl = key & 63, kb = kl >> 5, r = kl & 31;
    unsigned short* dst;
    float val;
    if (n < 96) {
      val = bfround(s * KSCALE);
      const int d = n, sidx = 2 * (d >> 5) + ((d >> 4) & 1), dd = d & 15, h = (dd >> 2) & 1, j = (dd & 3) + 4 * (dd >> 3);
      dst = (unsigned short*)(ws + WS_KVF + (size_t)t * TILE_BYTES + (kb * 6 + sidx) * 1024 + (r + 32 * h) * 16 + j * 2);
    } else {
      val = bfround(s);
      const int d = n - 96, db = d >> 5, rd = d & 31, sp = r >> 4, rr = r & 15, h = (rr >> 2) & 1, j = (rr & 3) + 4 * (rr >> 3);
      dst = (unsigned short*)(ws + WS_KVF + (size_t)t * TILE_BYTES + (12 + (kb * 2 + sp) * 3 + db) * 1024 + (rd + 32 * h) * 16 + j * 2);
    }
    *dst = (unsigned short)(__float_as_uint(val) >> 16);
    kvf[idx] = val;
  }
  __syncthreads();
  if (tid < 8) {
    float kn = 0.f, va = 0.f;
    for (int c = 0; c < 96; ++c) { const float kk = kvf[tid * 192 + c]; kn = fmaf(kk, kk, kn); va = fmaxf(va, fabsf(kvf[tid * 192 + 96 + c])); }
    float chk = 0.f;
    for (int c = 0; c < 96; ++c) chk += kvf[tid * 192 + 96 + c] * 0.f;
    va += chk;
    const int key = wi * 48 + jg * 8 + tid;
    ((float*)(ws + WS_KN2))[key] = kn;
    ((float*)(ws + WS_VAM))[key] = va;
  }
  if (b < 36) {
#pragma unroll
    for (int k = 0; k < 2; ++k) {
      const int e = tid + 256 * k, lane = e >> 3, j = e & 7, r = lane & 31, h = lane >> 5;
      unsigned short v;
      if (b < 18) {
        const int db = b / 6, ks = b % 6;
        v = wq[(16 * ks + 8 * h + j) * 96 + 32 * db + r];
        ((unsigned short*)(ws + WS_WQF))[b * 512 + e] = v;
      } else {
        const int bb = b - 18, ob = bb / 6, s = bb % 6;
        const int d = 32 * (s >> 1) + 16 * (s & 1) + 8 * (j >> 2) + 4 * h + (j & 3);
        v = wo[d * 96 + 32 * ob + r];
        ((unsigned short*)(ws + WS_WOF))[bb * 512 + e] = v;
      }
    }
  }
}

constexpr int RING = 3;
constexpr int LDS_W = RING * TILE_BYTES;
constexpr int LDS_MISC = LDS_W + 36 * 1024;
constexpr int LDS_BYTES = LDS_MISC + 256;

DI void dma16(const char* gsrc, unsigned lds_dst) {
  unsigned keep;
  asm volatile("s_mov_b32 %0, m0\n\ts_mov_b32 m0, %2\n\ts_nop 0\n\tglobal_load_lds_dwordx4 %1, off\n\ts_mov_b32 m0, %0"
               : "=&s"(keep) : "v"(gsrc), "s"(lds_dst) : "memory");
}

DI bf16x8 pack8(const f32x16& s, int base) {
  u32x4 w;
  w[0] = cvtpk(s[base + 0], s[base + 1]); w[1] = cvtpk(s[base + 2], s[base + 3]);
  w[2] = cvtpk(s[base + 4], s[base + 5]); w[3] = cvtpk(s[base + 6], s[base + 7]);
  return __builtin_bit_cast(bf16x8, w);
}
DI float swap32_add(float v) {
  auto r = __builtin_amdgcn_permlane32_swap(__float_as_uint(v), __float_as_uint(v), false, false);
  return __uint_as_float(r[0]) + __uint_as_float(r[1]);
}
DI float swap32_max(float v) {
  auto r = __builtin_amdgcn_permlane32_swap(__float_as_uint(v), __float_as_uint(v), false, false);
  return fmaxf(__uint_as_float(r[0]), __uint_as_float(r[1]));
}

template <int MODE>
DI void key_pass(const bool active, const char* kvf_lane, const unsigned ring_dma, const char* smem_lane,
                 const bf16x8 (&qf)[6], const f32x16& negm, f32x16 (&o)[3], float& lsum, float& rmax) {
  int slot = 0;
  for (int t = 0; t < NTILE; ++t) {
    asm volatile("s_waitcnt vmcnt(3)\n\ts_waitcnt lgkmcnt(0)" ::: "memory");
    __builtin_amdgcn_s_barrier();
    {
      int tn = t + 2; if (tn >= NTILE) tn -= NTILE;
      int sn = slot + 2; if (sn >= RING) sn -= RING;
      const char* g = kvf_lane + (size_t)tn * TILE_BYTES;
      const unsigned l = ring_dma + (unsigned)sn * TILE_BYTES;
      dma16(g, l); dma16(g + 1024, l + 1024); dma16(g + 2048, l + 2048);
    }
    if (active) {
      const char* sl = smem_lane + slot * TILE_BYTES;
      f32x16 s0, s1;
      if constexpr (MODE == 0) { s0 = negm; s1 = negm; }
      else {
#pragma unroll
        for (int r = 0; r < 16; ++r) { s0[r] = 0.f; s1[r] = 0.f; }
      }
#pragma unroll
      for (int s = 0; s < 6; ++s) {
        const bf16x8 k0 = *reinterpret_cast<const bf16x8*>(sl + s * 1024);
        const bf16x8 k1 = *reinterpret_cast<const bf16x8*>(sl + (6 + s) * 1024);
        s0 = MFMA32(k0, qf[s], s0);
        s1 = MFMA32(k1, qf[s], s1);
      }
      if constexpr (MODE == 0) {
#pragma unroll
        for (int r = 0; r < 16; ++r) { s0[r] = __builtin_amdgcn_exp2f(s0[r]); s1[r] = __builtin_amdgcn_exp2f(s1[r]); }
        float a0 = 0.f, a1 = 0.f;
#pragma unroll
        for (int r = 0; r < 16; ++r) { a0 += s0[r]; a1 += s1[r]; }
        lsum += a0 + a1;
        bf16x8 pk[4];
        pk[0] = pack8(s0, 0); pk[1] = pack8(s0, 8); pk[2] = pack8(s1, 0); pk[3] = pack8(s1, 8);
#pragma unroll
        for (int kbs = 0; kbs < 4; ++kbs)
#pragma unroll
          for (int db = 0; db < 3; ++db) {
            const bf16x8 vf = *reinterpret_cast<const bf16x8*>(sl + (12 + kbs * 3 + db) * 1024);
            o[db] = MFMA32(vf, pk[kbs], o[db]);
          }
      } else {
#pragma unroll
        for (int r = 0; r < 16; ++r) rmax = fmaxf(rmax, fmaxf(s0[r], s1[r]));
      }
    }
    ++slot; if (slot >= RING) slot = 0;
  }
}

__global__ __launch_bounds__(512, 2) void attn_kernel(const unsigned short* __restrict__ x, const char* __restrict__ ws,
                                                      unsigned short* __restrict__ out) {
  extern __shared__ __attribute__((aligned(16))) char smem[];
  const int tid = threadIdx.x, lane = tid & 63;
  const int wid = __builtin_amdgcn_readfirstlane(tid >> 6);
  const int r32 = lane & 31, hi = lane >> 5;
  const int wg = blockIdx.x;
  const char* kvf_lane = ws + WS_KVF + wid * 3072 + lane * 16;
  const unsigned ring_dma = (unsigned)(uintptr_t)smem + (unsigned)wid * 3072u;
  const char* smem_lane = smem + lane * 16;
  float* misc = (float*)(smem + LDS_MISC);

  dma16(kvf_lane, ring_dma); dma16(kvf_lane + 1024, ring_dma + 1024); dma16(kvf_lane + 2048, ring_dma + 2048);
  dma16(kvf_lane + TILE_BYTES, ring_dma + TILE_BYTES); dma16(kvf_lane + TILE_BYTES + 1024, ring_dma + TILE_BYTES + 1024);
  dma16(kvf_lane + TILE_BYTES + 2048, ring_dma + TILE_BYTES + 2048);

  for (int p = wid; p < 36; p += 8)
    dma16(ws + WS_WQF + p * 1024 + lane * 16, (unsigned)(uintptr_t)smem + LDS_W + p * 1024);
  float kn = 0.f, va = 0.f;
  {
    const float* kn2 = (const float*)(ws + WS_KN2);
    const float* vam = (const float*)(ws + WS_VAM);
    for (int i = tid; i < NKEY; i += 512) { kn = fmaxf(kn, kn2[i]); const float v = vam[i]; va = (v > va || v != v) ? v : va; }
#pragma unroll
    for (int off = 32; off >= 1; off >>= 1) {
      kn = fmaxf(kn, __shfl_xor(kn, off));
      const float v = __shfl_xor(va, off); va = (v > va || v != v) ? v : va;
    }
    if (lane == 0) { misc[wid] = kn; misc[8 + wid] = va; }
    asm volatile("s_waitcnt vmcnt(0)" ::: "memory");
    __syncthreads();
    kn = misc[0]; va = misc[8];
#pragma unroll
    for (int i = 1; i < 8; ++i) { kn = fmaxf(kn, misc[i]); const float v = misc[8 + i]; va = (v > va || v != v) ? v : va; }
    __syncthreads();
  }
  const float kmax = sqrtf(kn) * 1.001f + 1e-6f;
  float shift = 64.f;
  {
    const float lv = (va > 0.f) ? __log2f(va) : -200.f;
    float sh = floorf(110.f - lv);
    if (!(sh < 64.f)) sh = 64.f;
    if (!(sh > 0.f)) sh = 0.f;
    shift = sh;
  }

  f32x16 o[3]; bf16x8 qf[6]; f32x16 negm;
#pragma unroll
  for (int r = 0; r < 16; ++r) negm[r] = 0.f;
#pragma unroll
  for (int s = 0; s < 6; ++s) qf[s] = bf16x8{0, 0, 0, 0, 0, 0, 0, 0};

  for (int round = 0; round < 3; ++round) {
    const bool active = (round < 2) || (wid < 2);
    const int row0 = (round < 2) ? (wg * 512 + round * 256 + wid * 32) : (131072 + wg * 64 + wid * 32);
    float lsum = 0.f, rmax = -3.0e38f;
    if (active) {
      f32x16 qa[3];
#pragma unroll
      for (int db = 0; db < 3; ++db)
#pragma unroll
        for (int r = 0; r < 16; ++r) qa[db][r] = 0.f;
      const unsigned short* xrow = x + (size_t)(row0 + r32) * CH + hi * 8;
      const char* wqf = smem + LDS_W + lane * 16;
#pragma unroll
      for (int ks = 0; ks < 6; ++ks) {
        const bf16x8 xb = *reinterpret_cast<const bf16x8*>(xrow + ks * 16);
#pragma unroll
        for (int db = 0; db < 3; ++db) {
          const bf16x8 wa = *reinterpret_cast<const bf16x8*>(wqf + (db * 6 + ks) * 1024);
          qa[db] = MFMA32(wa, xb, qa[db]);
        }
      }
      float qn = 0.f;
#pragma unroll
      for (int s = 0; s < 6; ++s) {
        qf[s] = pack8(qa[s >> 1], 8 * (s & 1));
        const u32x4 w = __builtin_bit_cast(u32x4, qf[s]);
#pragma unroll
        for (int e = 0; e < 4; ++e) {
          const float a = __uint_as_float(w[e] << 16), bb = __uint_as_float(w[e] & 0xffff0000u);
          qn = fmaf(a, a, qn); qn = fmaf(bb, bb, qn);
        }
      }
      qn = swap32_add(qn);
      const float mb = sqrtf(qn) * kmax * 1.001f + 0.01f - shift;
#pragma unroll
      for (int r = 0; r < 16; ++r) negm[r] = -mb;
    }
#pragma unroll
    for (int db = 0; db < 3; ++db)
#pragma unroll
      for (int r = 0; r < 16; ++r) o[db][r] = 0.f;

    key_pass<0>(active, kvf_lane, ring_dma, smem_lane, qf, negm, o, lsum, rmax);

    float ltot = swap32_add(lsum);
    {
      const bool bad = active && !(ltot >= 8.673617379884035e-19f);
      if (tid == 0) misc[16] = 0.f;
      __syncthreads();
      if (bad) misc[16] = 1.f;
      __syncthreads();
      const float flag = misc[16];
      __syncthreads();
      if (flag != 0.f) {
        f32x16 dummy_o[3];
        float dl = 0.f;
        key_pass<1>(active, kvf_lane, ring_dma, smem_lane, qf, negm, dummy_o, dl, rmax);
        rmax = swap32_max(rmax);
#pragma unroll
        for (int r = 0; r < 16; ++r) negm[r] = -rmax;
#pragma unroll
        for (int db = 0; db < 3; ++db)
#pragma unroll
          for (int r = 0; r < 16; ++r) o[db][r] = 0.f;
        lsum = 0.f;
        key_pass<0>(active, kvf_lane, ring_dma, smem_lane, qf, negm, o, lsum, rmax);
        ltot = swap32_add(lsum);
      }
    }
    if (active) {
      const float inv = 1.0f / ltot;
      bf16x8 on[6];
#pragma unroll
      for (int db = 0; db < 3; ++db)
#pragma unroll
        for (int r = 0; r < 16; ++r) o[db][r] *= inv;
#pragma unroll
      for (int s = 0; s < 6; ++s) on[s] = pack8(o[s >> 1], 8 * (s & 1));
      const char* wof = smem + LDS_W + 18 * 1024 + lane * 16;
      unsigned short* orow = out + (size_t)(row0 + r32) * CH + 4 * hi;
#pragma unroll
      for (int ob = 0; ob < 3; ++ob) {
        f32x16 acc;
#pragma unroll
        for (int r = 0; r < 16; ++r) acc[r] = 0.f;
#pragma unroll
        for (int s = 0; s < 6; ++s) {
          const bf16x8 wa = *reinterpret_cast<const bf16x8*>(wof + (ob * 6 + s) * 1024);
          acc = MFMA32(wa, on[s], acc);
        }
#pragma unroll
        for (int g = 0; g < 4; ++g) {
          u32x2 w;
          w[0] = cvtpk(acc[4 * g], acc[4 * g + 1]); w[1] = cvtpk(acc[4 * g + 2], acc[4 * g + 3]);
          *reinterpret_cast<u32x2*>(orow + 32 * ob + 8 * g) = w;
        }
      }
    }
  }
  asm volatile("s_waitcnt vmcnt(0)" ::: "memory");
}

extern "C" void kernel_launch(void* const* d_in, const int* in_sizes, int n_in, void* d_out, int out_size, void* d_ws,
                              size_t ws_size, hipStream_t stream) {
  const unsigned short* x = (const unsigned short*)d_in[0];
  const unsigned short* wq = (const unsigned short*)d_in[1];
  const unsigned short* wkv = (const unsigned short*)d_in[2];
  const unsigned short* wo = (const unsigned short*)d_in[3];
  hipFuncSetAttribute((const void*)attn_kernel, hipFuncAttributeMaxDynamicSharedMemorySize, LDS_BYTES);
  hipLaunchKernelGGL(prep_kernel, dim3(288), dim3(256), 0, stream, x, wq, wkv, wo, (char*)d_ws);
  hipLaunchKernelGGL(attn_kernel, dim3(256), dim3(512), LDS_BYTES, stream, x, (const char*)d_ws, (unsigned short*)d_out);
}
```

```cpp
#include <hip/hip_runtime.h>
#include <hip/hip_bf16.h>
#include <stdint.h>
#include <utility>

typedef __attribute__((ext_vector_type(8))) short bf16x8;
typedef __attribute__((ext_vector_type(16))) float f32x16;
typedef __attribute__((ext_vector_type(4))) unsigned u32x4;
typedef __attribute__((ext_vector_type(2))) float f32x2;
typedef __attribute__((ext_vector_type(2))) __bf16 bf16x2_t;
typedef __attribute__((ext_vector_type(2))) unsigned u32x2;

#define DI __device__ __forceinline__
#define MFMA32(a, b, c) __builtin_amdgcn_mfma_f32_32x32x16_bf16((a), (b), (c), 0, 0, 0)

constexpr int IMG = 384, CH = 96, NPIX = IMG * IMG;
constexpr int NKEY = 2304, NTILE = 36;
constexpr int TILE_BYTES = 24576;
constexpr float KSCALE = 0.10206207261596575f * 1.4426950408889634f;

constexpr size_t WS_KVF = 0;
constexpr size_t WS_KN2 = WS_KVF + (size_t)NTILE * TILE_BYTES;
constexpr size_t WS_VAM = WS_KN2 + NKEY * 4;
constexpr size_t WS_WQF = WS_VAM + NKEY * 4;
constexpr size_t WS_WOF = WS_WQF + 18 * 1024;

DI unsigned cvtpk(float lo, float hi) {
  f32x2 v = {lo, hi};
  bf16x2_t b = __builtin_convertvector(v, bf16x2_t);
  return __builtin_bit_cast(unsigned, b);
}
DI float bf2f(unsigned short u) { return __uint_as_float(((unsigned)u) << 16); }
DI float bfround(float x) { return __uint_as_float(cvtpk(x, 0.f) << 16); }

__global__ __launch_bounds__(256) void prep_kernel(const unsigned short* __restrict__ x, const unsigned short* __restrict__ wq,
                                                   const unsigned short* __restrict__ wkv, const unsigned short* __restrict__ wo,
                                                   char* __restrict__ ws) {
  __shared__ __attribute__((aligned(16))) float part[64 * 96];
  __shared__ float xsf[8 * 96];
  __shared__ float kvf[8 * 192];
  const int tid = threadIdx.x, b = blockIdx.x;
  const int wi = b / 6, jg = b % 6;
#pragma unroll
  for (int k = 0; k < 3; ++k) {
    const int ci = tid + 256 * k, p = ci / 12, c = ci % 12;
    float acc[8];
#pragma unroll
    for (int e = 0; e < 8; ++e) acc[e] = 0.f;
#pragma unroll
    for (int rr = 0; rr < 8; ++rr) {
      const u32x4 v = *reinterpret_cast<const u32x4*>(x + ((size_t)(8 * wi + rr) * IMG + jg * 64 + p) * CH + c * 8);
#pragma unroll
      for (int e = 0; e < 4; ++e) {
        acc[2 * e] += __uint_as_float(v[e] << 16);
        acc[2 * e + 1] += __uint_as_float(v[e] & 0xffff0000u);
      }
    }
#pragma unroll
    for (int e = 0; e < 8; ++e) part[p * 96 + c * 8 + e] = acc[e];
  }
  __syncthreads();
#pragma unroll
  for (int k = 0; k < 3; ++k) {
    const int idx = tid + 256 * k, w = idx / 96, ch = idx % 96;
    float s = 0.f;
#pragma unroll
    for (int pp = 0; pp < 8; ++pp) s += part[(8 * w + pp) * 96 + ch];
    xsf[idx] = bfround(s * (1.0f / 64.0f));
  }
  __syncthreads();
  if (tid < 192) {
    const int n = tid;
    float acc[8];
#pragma unroll
    for (int w = 0; w < 8; ++w) acc[w] = 0.f;
#pragma unroll 8
    for (int c = 0; c < 96; ++c) {
      const float wv = bf2f(wkv[c * 192 + n]);
#pragma unroll
      for (int w = 0; w < 8; ++w) acc[w] = fmaf(xsf[w * 96 + c], wv, acc[w]);
    }
#pragma unroll
    for (int w = 0; w < 8; ++w) {
      const int key = wi * 48 + jg * 8 + w, t = key >> 6, kl = key & 63, kb = kl >> 5, r = kl & 31;
      unsigned short* dst;
      float val;
      if (n < 96) {
        val = bfround(acc[w] * KSCALE);
        const int d = n, sidx = 2 * (d >> 5) + ((d >> 4) & 1), dd = d & 15, h = (dd >> 2) & 1, j = (dd & 3) + 4 * (dd >> 3);
        dst = (unsigned short*)(ws + WS_KVF + (size_t)t * TILE_BYTES + (kb * 6 + sidx) * 1024 + (r + 32 * h) * 16 + j * 2);
      } else {
        val = bfround(acc[w]);
        const int d = n - 96, db = d >> 5, rd = d & 31, sp = r >> 4, rr = r & 15, h = (rr >> 2) & 1, j = (rr & 3) + 4 * (rr >> 3);
        dst = (unsigned short*)(ws + WS_KVF + (size_t)t * TILE_BYTES + (12 + (kb * 2 + sp) * 3 + db) * 1024 + (rd + 32 * h) * 16 + j * 2);
      }
      *dst = (unsigned short)(__float_as_uint(val) >> 16);
      kvf[w * 192 + n] = val;
    }
  }
  __syncthreads();
  {
    const int wv = tid >> 6, ln = tid & 63;
#pragma unroll
    for (int ww = 0; ww < 2; ++ww) {
      const int w = 2 * wv + ww;
      float kn = 0.f, va = 0.f, chk = 0.f;
      for (int c = ln; c < 96; c += 64) {
        const float kk = kvf[w * 192 + c], vv = kvf[w * 192 + 96 + c];
        kn = fmaf(kk, kk, kn); va = fmaxf(va, fabsf(vv)); chk += vv * 0.f;
      }
#pragma unroll
      for (int off = 32; off >= 1; off >>= 1) {
        kn += __shfl_xor(kn, off); va = fmaxf(va, __shfl_xor(va, off)); chk += __shfl_xor(chk, off);
      }
      if (ln == 0) {
        const int key = wi * 48 + jg * 8 + w;
        ((float*)(ws + WS_KN2))[key] = kn;
        ((float*)(ws + WS_VAM))[key] = va + chk;
      }
    }
  }
  if (b < 36) {
#pragma unroll
    for (int k = 0; k < 2; ++k) {
      const int e = tid + 256 * k, lane = e >> 3, j = e & 7, r = lane & 31, h = lane >> 5;
      unsigned short v;
      if (b < 18) {
        const int db = b / 6, ks = b % 6;
        v = wq[(16 * ks + 8 * h + j) * 96 + 32 * db + r];
        ((unsigned short*)(ws + WS_WQF))[b * 512 + e] = v;
      } else {
        const int bb = b - 18, ob = bb / 6, s = bb % 6;
        const int d = 32 * (s >> 1) + 16 * (s & 1) + 8 * (j >> 2) + 4 * h + (j & 3);
        v = wo[d * 96 + 32 * ob + r];
        ((unsigned short*)(ws + WS_WOF))[bb * 512 + e] = v;
      }
    }
  }
}

constexpr int RING = 4;
constexpr int LDS_W = RING * TILE_BYTES;
constexpr int LDS_MISC = LDS_W + 36 * 1024;
constexpr int LDS_CMB = LDS_MISC + 256;
constexpr int LDS_BYTES = LDS_CMB + 2 * 64 * 49 * 4;

DI void dma16(const char* gsrc, unsigned lds_dst) {
  unsigned keep;
  asm volatile("s_mov_b32 %0, m0\n\ts_mov_b32 m0, %2\n\ts_nop 0\n\tglobal_load_lds_dwordx4 %1, off\n\ts_mov_b32 m0, %0"
               : "=&s"(keep) : "v"(gsrc), "s"(lds_dst) : "memory");
}
DI bf16x8 pack8(const f32x16& s, int base) {
  u32x4 w;
  w[0] = cvtpk(s[base + 0], s[base + 1]); w[1] = cvtpk(s[base + 2], s[base + 3]);
  w[2] = cvtpk(s[base + 4], s[base + 5]); w[3] = cvtpk(s[base + 6], s[base + 7]);
  return __builtin_bit_cast(bf16x8, w);
}
DI float swap32_add(float v) {
  auto r = __builtin_amdgcn_permlane32_swap(__float_as_uint(v), __float_as_uint(v), false, false);
  return __uint_as_float(r[0]) + __uint_as_float(r[1]);
}
DI float swap32_max(float v) {
  auto r = __builtin_amdgcn_permlane32_swap(__float_as_uint(v), __float_as_uint(v), false, false);
  return fmaxf(__uint_as_float(r[0]), __uint_as_float(r[1]));
}
DI void mfma_v_init(f32x16& d, const bf16x8& a, const bf16x8& b, const f32x16& c) {
  asm volatile("v_mfma_f32_32x32x16_bf16 %0, %1, %2, %3" : "=&v"(d) : "v"(a), "v"(b), "v"(c));
}
DI void mfma_v_acc(f32x16& d, const bf16x8& a, const bf16x8& b) {
  asm volatile("v_mfma_f32_32x32x16_bf16 %0, %1, %2, %0" : "+v"(d) : "v"(a), "v"(b));
}
DI float fadd_s(float a, float b) { float r; asm("v_add_f32_e32 %0, %1, %2" : "=v"(r) : "v"(a), "v"(b)); return r; }
#define SB0() __builtin_amdgcn_sched_barrier(0)
#define LDSFRAG(base, i) (*reinterpret_cast<const bf16x8*>((base) + (i) * 1024))

template <int NQB> struct KState {
  f32x16 sA[NQB], sB[NQB];
  bf16x8 pkA[NQB][2], pkB[NQB][2];
  bf16x8 kfA[6], kfB[6], vfA[6], vfB[6];
  float l0[NQB], l1[NQB];
};

template <int NQB, int H, int G, int NG>
DI void sm_slice(f32x16 (&sx)[NQB], bf16x8 (&pk)[NQB][2], float (&l0)[NQB], float (&l1)[NQB]) {
  constexpr int NE = 8 * NQB;
  constexpr int NGE = NG - 1;
  constexpr int lo = (G < NGE) ? (G * NE) / NGE : NE, hi = (G < NGE) ? ((G + 1) * NE) / NGE : NE;
  constexpr int plo = (G >= 1) ? (((G - 1) < NGE) ? ((G - 1) * NE) / NGE : NE) : 0, phi = (G >= 1) ? lo : 0;
  if constexpr (H == 0 && G == 0) asm volatile("s_nop 7\n\ts_nop 3" : "+v"(sx[0]), "+v"(sx[NQB - 1]));
#pragma unroll
  for (int e = lo; e < hi; ++e) {
    const int ee = H * NE + e, qb = ee >> 4, r = ee & 15;
    sx[qb][r] = __builtin_amdgcn_exp2f(sx[qb][r]);
  }
#pragma unroll
  for (int e = plo; e < phi; ++e) {
    const int ee = H * NE + e, qb = ee >> 4, r = ee & 15;
    if (r & 1) l1[qb] = fadd_s(l1[qb], sx[qb][r]); else l0[qb] = fadd_s(l0[qb], sx[qb][r]);
    if (r & 1) {
      u32x4 w = __builtin_bit_cast(u32x4, pk[qb][r >> 3]);
      w[(r >> 1) & 3] = cvtpk(sx[qb][r - 1], sx[qb][r]);
      pk[qb][r >> 3] = __builtin_bit_cast(bf16x8, w);
    }
  }
}

template <int NQB, bool DMA, bool DOM, bool DOS, bool DOR, int G>
DI void qk_gap(f32x16 (&sx)[NQB], const bf16x8 (&kf)[6], const bf16x8 (&qf)[NQB][6], const f32x16 (&negm)[NQB],
               f32x16 (&so)[NQB], bf16x8 (&pko)[NQB][2], float (&l0)[NQB], float (&l1)[NQB],
               bf16x8 (&rd)[6], const char* rdbase, const char* g, unsigned l) {
  constexpr int NG = 6 * NQB, s = G / NQB, qb = G % NQB;
  if constexpr (DOM) {
    if constexpr (s == 0) mfma_v_init(sx[qb], kf[0], qf[qb][0], negm[qb]);
    else mfma_v_acc(sx[qb], kf[s], qf[qb][s]);
  }
  if constexpr (DOR && G < 6) rd[G] = LDSFRAG(rdbase, G);
  if constexpr (DMA && G >= NG - 3) dma16(g + (G - (NG - 3)) * 1024, l + (G - (NG - 3)) * 1024);
  if constexpr (DOS) sm_slice<NQB, 1, G, NG>(so, pko, l0, l1);
  SB0();
}
template <int NQB, bool DOM, bool DOS, bool DOR, int G>
DI void pv_gap(f32x16 (&o)[NQB][3], const bf16x8 (&vf)[6], const bf16x8 (&pk)[NQB][2],
               f32x16 (&sn)[NQB], bf16x8 (&pkn)[NQB][2], float (&l0)[NQB], float (&l1)[NQB],
               bf16x8 (&rd)[6], const char* rdbase) {
  constexpr int NG = 6 * NQB, kk = G / (3 * NQB), rem = G % (3 * NQB), db = rem / NQB, qb = rem % NQB;
  if constexpr (DOM) o[qb][db] = MFMA32(vf[kk * 3 + db], pk[qb][kk], o[qb][db]);
  if constexpr (DOR && G < 6) rd[G] = LDSFRAG(rdbase, G);
  if constexpr (DOS) sm_slice<NQB, 0, G, NG>(sn, pkn, l0, l1);
  SB0();
}
template <int NQB, bool DMA, bool DOM, bool DOS, bool DOR, int... Gs, typename... A> DI void qk_phase(std::integer_sequence<int, Gs...>, A&&... a) { (qk_gap<NQB, DMA, DOM, DOS, DOR, Gs>(a...), ...); }
template <int NQB, bool DOM, bool DOS, bool DOR, int... Gs, typename... A> DI void pv_phase(std::integer_sequence<int, Gs...>, A&&... a) { (pv_gap<NQB, DOM, DOS, DOR, Gs>(a...), ...); }

template <int NQB, int HALF = 0>
DI void key_pass(const char* kvf_lane, const unsigned ring_dma, const char* smem_lane,
                 const bf16x8 (&qf)[NQB ? NQB : 1][6], const f32x16 (&negm)[NQB ? NQB : 1], f32x16 (&o)[NQB ? NQB : 1][3], float (&lsum)[NQB ? NQB : 1]) {
  constexpr int NQ = NQB ? NQB : 1;
  asm volatile("s_waitcnt vmcnt(6)\n\ts_waitcnt lgkmcnt(0)" ::: "memory");
  __builtin_amdgcn_s_barrier();
  if constexpr (NQB == 0) {
    int slot = 0;
    for (int t = 0; t < NTILE; ++t) {
      asm volatile("s_waitcnt vmcnt(3)" ::: "memory");
      __builtin_amdgcn_s_barrier();
      int tn = t + 3; if (tn >= NTILE) tn -= NTILE;
      const int sn = (slot + 3) & 3;
      const char* g = kvf_lane + (size_t)tn * TILE_BYTES;
      const unsigned l = ring_dma + (unsigned)sn * TILE_BYTES;
#pragma unroll
      for (int i = 0; i < 3; ++i) dma16(g + i * 1024, l + i * 1024);
      slot = (slot + 1) & 3;
    }
  } else {
    KState<NQ> k;
    using seq = std::make_integer_sequence<int, 6 * NQ>;
#pragma unroll
    for (int q = 0; q < NQ; ++q) {
      k.l0[q] = 0.f; k.l1[q] = 0.f;
#pragma unroll
      for (int r = 0; r < 16; ++r) { k.sB[q][r] = -1.0e30f; }
#pragma unroll
      for (int i = 0; i < 2; ++i) k.pkB[q][i] = bf16x8{0, 0, 0, 0, 0, 0, 0, 0};
    }
#pragma unroll
    for (int i = 0; i < 6; ++i) { k.vfB[i] = bf16x8{0, 0, 0, 0, 0, 0, 0, 0}; k.kfA[i] = LDSFRAG(smem_lane, i); }
    int slot = 0;
    for (int t = 0; t < NTILE; ++t) {
      const char* sl = smem_lane + slot * TILE_BYTES;
      const int nslot = (slot + 1) & 3;
      constexpr bool DA = (HALF != 2), DB = (HALF != 1);
      qk_phase<NQ, false, DA, DB, DB>(seq{}, k.sA, k.kfA, qf, negm, k.sB, k.pkB, k.l0, k.l1, k.kfB, sl + 6 * 1024, (const char*)nullptr, 0u);
      pv_phase<NQ, DB, DA, DA>(seq{}, o, k.vfB, k.pkB, k.sA, k.pkA, k.l0, k.l1, k.vfA, sl + 12 * 1024);
      asm volatile("s_waitcnt vmcnt(3)\n\ts_waitcnt lgkmcnt(0)" ::: "memory");
      __builtin_amdgcn_s_barrier();
      int tn = t + 3; if (tn >= NTILE) tn -= NTILE;
      const char* g = kvf_lane + (size_t)tn * TILE_BYTES;
      const unsigned l = ring_dma + (unsigned)((slot + 3) & 3) * TILE_BYTES;
      qk_phase<NQ, true, DB, DA, DA>(seq{}, k.sB, k.kfB, qf, negm, k.sA, k.pkA, k.l0, k.l1, k.kfA, smem_lane + nslot * TILE_BYTES, g, l);
      pv_phase<NQ, DA, DB, DB>(seq{}, o, k.vfA, k.pkA, k.sB, k.pkB, k.l0, k.l1, k.vfB, sl + 18 * 1024);
      slot = nslot;
    }
    if constexpr (HALF != 1) {
#pragma unroll
    for (int e = 0; e < 8 * NQ; ++e) {
      const int ee = 8 * NQ + e, qb = ee >> 4, r = ee & 15;
      k.sB[qb][r] = __builtin_amdgcn_exp2f(k.sB[qb][r]);
      if (r & 1) k.l1[qb] += k.sB[qb][r]; else k.l0[qb] += k.sB[qb][r];
    }
    if constexpr (NQ == 2) { k.pkB[1][0] = pack8(k.sB[1], 0); k.pkB[1][1] = pack8(k.sB[1], 8); }
    else k.pkB[0][1] = pack8(k.sB[0], 8);
#pragma unroll
    for (int kk = 0; kk < 2; ++kk)
#pragma unroll
      for (int db = 0; db < 3; ++db)
#pragma unroll
        for (int qb = 0; qb < NQ; ++qb) o[qb][db] = MFMA32(k.vfB[kk * 3 + db], k.pkB[qb][kk], o[qb][db]);
    }
#pragma unroll
    for (int q = 0; q < NQ; ++q) lsum[q] = k.l0[q] + k.l1[q];
  }
}

template <int NQB>
DI void max_pass(const char* kvf_lane, const unsigned ring_dma, const char* smem_lane,
                 const bf16x8 (&qf)[NQB ? NQB : 1][6], float (&rmax)[NQB ? NQB : 1]) {
  asm volatile("s_waitcnt vmcnt(6)\n\ts_waitcnt lgkmcnt(0)" ::: "memory");
  __builtin_amdgcn_s_barrier();
  int slot = 0;
  for (int t = 0; t < NTILE; ++t) {
    if constexpr (NQB > 0) {
      const char* sl = smem_lane + slot * TILE_BYTES;
#pragma unroll
      for (int kb = 0; kb < 2; ++kb)
#pragma unroll
        for (int q = 0; q < NQB; ++q) {
          f32x16 s;
#pragma unroll
          for (int r = 0; r < 16; ++r) s[r] = 0.f;
#pragma unroll
          for (int ss = 0; ss < 6; ++ss) s = MFMA32(LDSFRAG(sl, kb * 6 + ss), qf[q][ss], s);
#pragma unroll
          for (int r = 0; r < 16; ++r) rmax[q] = fmaxf(rmax[q], s[r]);
        }
    }
    asm volatile("s_waitcnt vmcnt(3)\n\ts_waitcnt lgkmcnt(0)" ::: "memory");
    __builtin_amdgcn_s_barrier();
    int tn = t + 3; if (tn >= NTILE) tn -= NTILE;
    const char* g = kvf_lane + (size_t)tn * TILE_BYTES;
    const unsigned l = ring_dma + (unsigned)((slot + 3) & 3) * TILE_BYTES;
#pragma unroll
    for (int i = 0; i < 3; ++i) dma16(g + i * 1024, l + i * 1024);
    slot = (slot + 1) & 3;
  }
}

template <int NQB, int HALF = 0>
DI void do_round(const int row0, bf16x8 (&xb)[6], const unsigned short* __restrict__ xnext, unsigned short* __restrict__ out, char* smem,
                 const char* kvf_lane, const unsigned ring_dma, const int lane, const int tid, const int wid, const float kmax, const float shift, const bool split = false) {
  constexpr int NQ = NQB ? NQB : 1;
  const int r32 = lane & 31, hi = lane >> 5;
  const char* smem_lane = smem + lane * 16;
  float* misc = (float*)(smem + LDS_MISC);
  bf16x8 qf[NQ][6]; f32x16 negm[NQ]; f32x16 o[NQ][3]; float lsum[NQ];
  if constexpr (NQB > 0) {
    const char* wqf = smem + LDS_W + lane * 16;
#pragma unroll
    for (int q = 0; q < NQ; ++q) {
      f32x16 qa[3];
#pragma unroll
      for (int db = 0; db < 3; ++db)
#pragma unroll
        for (int r = 0; r < 16; ++r) qa[db][r] = 0.f;
#pragma unroll
      for (int ks = 0; ks < 6; ++ks) {
#pragma unroll
        for (int db = 0; db < 3; ++db) qa[db] = MFMA32(LDSFRAG(wqf, db * 6 + ks), xb[ks], qa[db]);
      }
      if (xnext) {
#pragma unroll
        for (int ks = 0; ks < 6; ++ks) xb[ks] = *reinterpret_cast<const bf16x8*>(xnext + ks * 16);
      }
      float qn = 0.f;
#pragma unroll
      for (int s = 0; s < 6; ++s) {
        qf[q][s] = pack8(qa[s >> 1], 8 * (s & 1));
        const u32x4 w = __builtin_bit_cast(u32x4, qf[q][s]);
#pragma unroll
        for (int e = 0; e < 4; ++e) {
          const float a = __uint_as_float(w[e] << 16), bb = __uint_as_float(w[e] & 0xffff0000u);
          qn = fmaf(a, a, qn); qn = fmaf(bb, bb, qn);
        }
      }
      qn = swap32_add(qn);
      const float mb = sqrtf(qn) * kmax * 1.001f + 0.01f - shift;
#pragma unroll
      for (int r = 0; r < 16; ++r) negm[q][r] = -mb;
#pragma unroll
      for (int db = 0; db < 3; ++db)
#pragma unroll
        for (int r = 0; r < 16; ++r) o[q][db][r] = 0.f;
    }
  }
  key_pass<NQB, HALF>(kvf_lane, ring_dma, smem_lane, qf, negm, o, lsum);
  if constexpr (HALF != 0) {
    float* cmb = (float*)(smem + LDS_CMB) + (wid & 1) * 64 * 49;
    if constexpr (HALF == 2) {
#pragma unroll
      for (int db = 0; db < 3; ++db)
#pragma unroll
        for (int r = 0; r < 16; ++r) cmb[(db * 16 + r) * 64 + lane] = o[0][db][r];
      cmb[48 * 64 + lane] = lsum[0];
    }
    __syncthreads();
    if constexpr (HALF == 1) {
#pragma unroll
      for (int db = 0; db < 3; ++db)
#pragma unroll
        for (int r = 0; r < 16; ++r) o[0][db][r] += cmb[(db * 16 + r) * 64 + lane];
      lsum[0] += cmb[48 * 64 + lane];
    }
    __syncthreads();
  } else if constexpr (NQB == 0) { if (split) { __syncthreads(); __syncthreads(); } }
  float ltot[NQ];
  bool bad = false;
  if constexpr (NQB > 0) {
#pragma unroll
    for (int q = 0; q < NQ; ++q) { ltot[q] = swap32_add(lsum[q]); bad = bad || (HALF != 2 && !(ltot[q] >= 8.673617379884035e-19f)); }
  }
  {
    const bool wbad = __any(bad);
    if (lane == 0) misc[16 + wid] = wbad ? 1.f : 0.f;
  }
  __syncthreads();
  float flag = 0.f;
#pragma unroll
  for (int i = 0; i < 8; ++i) flag += misc[16 + i];
  if (flag != 0.f) {
    float rmax[NQ];
#pragma unroll
    for (int q = 0; q < NQ; ++q) rmax[q] = -3.0e38f;
    max_pass<NQB>(kvf_lane, ring_dma, smem_lane, qf, rmax);
    if constexpr (NQB > 0) {
#pragma unroll
      for (int q = 0; q < NQ; ++q) {
        const float m = swap32_max(rmax[q]);
#pragma unroll
        for (int r = 0; r < 16; ++r) negm[q][r] = -m;
#pragma unroll
        for (int db = 0; db < 3; ++db)
#pragma unroll
          for (int r = 0; r < 16; ++r) o[q][db][r] = 0.f;
      }
    }
    key_pass<NQB, HALF>(kvf_lane, ring_dma, smem_lane, qf, negm, o, lsum);
  if constexpr (HALF != 0) {
    float* cmb = (float*)(smem + LDS_CMB) + (wid & 1) * 64 * 49;
    if constexpr (HALF == 2) {
#pragma unroll
      for (int db = 0; db < 3; ++db)
#pragma unroll
        for (int r = 0; r < 16; ++r) cmb[(db * 16 + r) * 64 + lane] = o[0][db][r];
      cmb[48 * 64 + lane] = lsum[0];
    }
    __syncthreads();
    if constexpr (HALF == 1) {
#pragma unroll
      for (int db = 0; db < 3; ++db)
#pragma unroll
        for (int r = 0; r < 16; ++r) o[0][db][r] += cmb[(db * 16 + r) * 64 + lane];
      lsum[0] += cmb[48 * 64 + lane];
    }
    __syncthreads();
  } else if constexpr (NQB == 0) { if (split) { __syncthreads(); __syncthreads(); } }
    if constexpr (NQB > 0) {
#pragma unroll
      for (int q = 0; q < NQ; ++q) ltot[q] = swap32_add(lsum[q]);
    }
  }
  if constexpr (NQB > 0 && HALF != 2) {
    const char* wof = smem + LDS_W + 18 * 1024 + lane * 16;
#pragma unroll
    for (int q = 0; q < NQ; ++q) {
      const float inv = 1.0f / ltot[q];
      bf16x8 on[6];
#pragma unroll
      for (int db = 0; db < 3; ++db)
#pragma unroll
        for (int r = 0; r < 16; ++r) o[q][db][r] *= inv;
#pragma unroll
      for (int s = 0; s < 6; ++s) on[s] = pack8(o[q][s >> 1], 8 * (s & 1));
      unsigned short* orow = out + (size_t)(row0 + q * 32 + r32) * CH + 4 * hi;
#pragma unroll
      for (int ob = 0; ob < 3; ++ob) {
        f32x16 acc;
#pragma unroll
        for (int r = 0; r < 16; ++r) acc[r] = 0.f;
#pragma unroll
        for (int s = 0; s < 6; ++s) acc = MFMA32(LDSFRAG(wof, ob * 6 + s), on[s], acc);
#pragma unroll
        for (int g = 0; g < 4; ++g) {
          u32x2 w;
          w[0] = cvtpk(acc[4 * g], acc[4 * g + 1]); w[1] = cvtpk(acc[4 * g + 2], acc[4 * g + 3]);
          *reinterpret_cast<u32x2*>(orow + 32 * ob + 8 * g) = w;
        }
      }
    }
  }
}

__global__ __launch_bounds__(512, 2) void attn_kernel(const unsigned short* __restrict__ x, const char* __restrict__ ws,
                                                      unsigned short* __restrict__ out) {
  extern __shared__ __attribute__((aligned(16))) char smem[];
  const int tid = threadIdx.x, lane = tid & 63;
  const int wid = __builtin_amdgcn_readfirstlane(tid >> 6);
  const int wg = blockIdx.x;
  const char* kvf_lane = ws + WS_KVF + wid * 3072 + lane * 16;
  const unsigned ring_dma = (unsigned)(uintptr_t)smem + (unsigned)wid * 3072u;
  float* misc = (float*)(smem + LDS_MISC);
  (void)misc;
#pragma unroll
  for (int t = 0; t < 3; ++t)
#pragma unroll
    for (int i = 0; i < 3; ++i) dma16(kvf_lane + t * TILE_BYTES + i * 1024, ring_dma + t * TILE_BYTES + i * 1024);
  for (int p = wid; p < 36; p += 8)
    dma16(ws + WS_WQF + p * 1024 + lane * 16, (unsigned)(uintptr_t)smem + LDS_W + p * 1024);
  bf16x8 xb[6];
  {
    const unsigned short* xr = x + (size_t)(wg * 512 + wid * 32 + (lane & 31)) * CH + (lane >> 5) * 8;
#pragma unroll
    for (int ks = 0; ks < 6; ++ks) xb[ks] = *reinterpret_cast<const bf16x8*>(xr + ks * 16);
  }
  float kn = 0.f, va = 0.f;
  {
    const float* kn2 = (const float*)(ws + WS_KN2);
    const float* vam = (const float*)(ws + WS_VAM);
    for (int i = tid; i < NKEY; i += 512) { kn = fmaxf(kn, kn2[i]); const float v = vam[i]; va = (v > va || v != v) ? v : va; }
#pragma unroll
    for (int off = 32; off >= 1; off >>= 1) {
      kn = fmaxf(kn, __shfl_xor(kn, off));
      const float v = __shfl_xor(va, off); va = (v > va || v != v) ? v : va;
    }
    if (lane == 0) { misc[wid] = kn; misc[8 + wid] = va; }
    __syncthreads();
    kn = misc[0]; va = misc[8];
#pragma unroll
    for (int i = 1; i < 8; ++i) { kn = fmaxf(kn, misc[i]); const float v = misc[8 + i]; va = (v > va || v != v) ? v : va; }
    __syncthreads();
  }
  const float kmax = sqrtf(kn) * 1.001f + 1e-6f;
  float shift;
  {
    const float lv = (va > 0.f) ? __log2f(va) : -200.f;
    float sh = floorf(110.f - lv);
    if (!(sh < 64.f)) sh = 64.f;
    if (!(sh > 0.f)) sh = 0.f;
    shift = sh;
  }
  for (int round = 0; round < 2; ++round) {
    const unsigned short* xn = (round == 0) ? x + (size_t)(wg * 512 + 256 + wid * 32 + (lane & 31)) * CH + (lane >> 5) * 8
                             : ((wid < 4) ? x + (size_t)(131072 + wg * 64 + (wid & 1) * 32 + (lane & 31)) * CH + (lane >> 5) * 8 : nullptr);
    do_round<1>(wg * 512 + round * 256 + wid * 32, xb, xn, out, smem, kvf_lane, ring_dma, lane, tid, wid, kmax, shift);
  }
  if (wid < 2) do_round<1>(131072 + wg * 64 + (wid & 1) * 32, xb, nullptr, out, smem, kvf_lane, ring_dma, lane, tid, wid, kmax, shift);
  else         do_round<0>(0, xb, nullptr, out, smem, kvf_lane, ring_dma, lane, tid, wid, kmax, shift);
  asm volatile("s_waitcnt vmcnt(0)" ::: "memory");
}

extern "C" void kernel_launch(void* const* d_in, const int* in_sizes, int n_in, void* d_out, int out_size, void* d_ws,
                              size_t ws_size, hipStream_t stream) {
  const unsigned short* x = (const unsigned short*)d_in[0];
  const unsigned short* wq = (const unsigned short*)d_in[1];
  const unsigned short* wkv = (const unsigned short*)d_in[2];
  const unsigned short* wo = (const unsigned short*)d_in[3];
  hipFuncSetAttribute((const void*)attn_kernel, hipFuncAttributeMaxDynamicSharedMemorySize, LDS_BYTES);
  hipLaunchKernelGGL(prep_kernel, dim3(288), dim3(256), 0, stream, x, wq, wkv, wo, (char*)d_ws);
  hipLaunchKernelGGL(attn_kernel, dim3(256), dim3(512), LDS_BYTES, stream, x, (const char*)d_ws, (unsigned short*)d_out);
}
```

```cpp
#include <hip/hip_runtime.h>
#include <hip/hip_bf16.h>
#include <stdint.h>
#include <utility>

typedef __attribute__((ext_vector_type(8))) short bf16x8;
typedef __attribute__((ext_vector_type(16))) float f32x16;
typedef __attribute__((ext_vector_type(4))) unsigned u32x4;
typedef __attribute__((ext_vector_type(2))) float f32x2;
typedef __attribute__((ext_vector_type(2))) __bf16 bf16x2_t;
typedef __attribute__((ext_vector_type(2))) unsigned u32x2;

#define DI __device__ __forceinline__
#define MFMA32(a, b, c) __builtin_amdgcn_mfma_f32_32x32x16_bf16((a), (b), (c), 0, 0, 0)

constexpr int IMG = 384, CH = 96, NPIX = IMG * IMG;
constexpr int NKEY = 2304, NTILE = 36;
constexpr int TILE_BYTES = 24576;
constexpr float KSCALE = 0.10206207261596575f * 1.4426950408889634f;

constexpr size_t WS_KVF = 0;
constexpr size_t WS_KN2 = WS_KVF + (size_t)NTILE * TILE_BYTES;
constexpr size_t WS_VAM = WS_KN2 + NKEY * 4;
constexpr size_t WS_WQF = WS_VAM + NKEY * 4;
constexpr size_t WS_WOF = WS_WQF + 18 * 1024;

DI unsigned cvtpk(float lo, float hi) {
  f32x2 v = {lo, hi};
  bf16x2_t b = __builtin_convertvector(v, bf16x2_t);
  return __builtin_bit_cast(unsigned, b);
}
DI float bf2f(unsigned short u) { return __uint_as_float(((unsigned)u) << 16); }
DI float bfround(float x) { return __uint_as_float(cvtpk(x, 0.f) << 16); }

__global__ __launch_bounds__(256) void prep_kernel(const unsigned short* __restrict__ x, const unsigned short* __restrict__ wq,
                                                   const unsigned short* __restrict__ wkv, const unsigned short* __restrict__ wo,
                                                   char* __restrict__ ws) {
  __shared__ __attribute__((aligned(16))) float part[64 * 96];
  __shared__ float xsf[8 * 96];
  __shared__ float kvf[8 * 192];
  const int tid = threadIdx.x, b = blockIdx.x;
  const int wi = b / 6, jg = b % 6;
#pragma unroll
  for (int k = 0; k < 3; ++k) {
    const int ci = tid + 256 * k, p = ci / 12, c = ci % 12;
    float acc[8];
#pragma unroll
    for (int e = 0; e < 8; ++e) acc[e] = 0.f;
#pragma unroll
    for (int rr = 0; rr < 8; ++rr) {
      const u32x4 v = *reinterpret_cast<const u32x4*>(x + ((size_t)(8 * wi + rr) * IMG + jg * 64 + p) * CH + c * 8);
#pragma unroll
      for (int e = 0; e < 4; ++e) {
        acc[2 * e] += __uint_as_float(v[e] << 16);
        acc[2 * e + 1] += __uint_as_float(v[e] & 0xffff0000u);
      }
    }
#pragma unroll
    for (int e = 0; e < 8; ++e) part[p * 96 + c * 8 + e] = acc[e];
  }
  __syncthreads();
#pragma unroll
  for (int k = 0; k < 3; ++k) {
    const int idx = tid + 256 * k, w = idx / 96, ch = idx % 96;
    float s = 0.f;
#pragma unroll
    for (int pp = 0; pp < 8; ++pp) s += part[(8 * w + pp) * 96 + ch];
    xsf[idx] = bfround(s * (1.0f / 64.0f));
  }
  __syncthreads();
  if (tid < 192) {
    const int n = tid;
    float acc[8];
#pragma unroll
    for (int w = 0; w < 8; ++w) acc[w] = 0.f;
#pragma unroll 8
    for (int c = 0; c < 96; ++c) {
      const float wv = bf2f(wkv[c * 192 + n]);
#pragma unroll
      for (int w = 0; w < 8; ++w) acc[w] = fmaf(xsf[w * 96 + c], wv, acc[w]);
    }
#pragma unroll
    for (int w = 0; w < 8; ++w) {
      const int key = wi * 48 + jg * 8 + w, t = key >> 6, kl = key & 63, kb = kl >> 5, r = kl & 31;
      unsigned short* dst;
      float val;
      if (n < 96) {
        val = bfround(acc[w] * KSCALE);
        const int d = n, sidx = 2 * (d >> 5) + ((d >> 4) & 1), dd = d & 15, h = (dd >> 2) & 1, j = (dd & 3) + 4 * (dd >> 3);
        dst = (unsigned short*)(ws + WS_KVF + (size_t)t * TILE_BYTES + (kb * 6 + sidx) * 1024 + (r + 32 * h) * 16 + j * 2);
      } else {
        val = bfround(acc[w]);
        const int d = n - 96, db = d >> 5, rd = d & 31, sp = r >> 4, rr = r & 15, h = (rr >> 2) & 1, j = (rr & 3) + 4 * (rr >> 3);
        dst = (unsigned short*)(ws + WS_KVF + (size_t)t * TILE_BYTES + (12 + (kb * 2 + sp) * 3 + db) * 1024 + (rd + 32 * h) * 16 + j * 2);
      }
      *dst = (unsigned short)(__float_as_uint(val) >> 16);
      kvf[w * 192 + n] = val;
    }
  }
  __syncthreads();
  {
    const int wv = tid >> 6, ln = tid & 63;
#pragma unroll
    for (int ww = 0; ww < 2; ++ww) {
      const int w = 2 * wv + ww;
      float kn = 0.f, va = 0.f, chk = 0.f;
      for (int c = ln; c < 96; c += 64) {
        const float kk = kvf[w * 192 + c], vv = kvf[w * 192 + 96 + c];
        kn = fmaf(kk, kk, kn); va = fmaxf(va, fabsf(vv)); chk += vv * 0.f;
      }
#pragma unroll
      for (int off = 32; off >= 1; off >>= 1) {
        kn += __shfl_xor(kn, off); va = fmaxf(va, __shfl_xor(va, off)); chk += __shfl_xor(chk, off);
      }
      if (ln == 0) {
        const int key = wi * 48 + jg * 8 + w;
        ((float*)(ws + WS_KN2))[key] = kn;
        ((float*)(ws + WS_VAM))[key] = va + chk;
      }
    }
  }
  if (b < 36) {
#pragma unroll
    for (int k = 0; k < 2; ++k) {
      const int e = tid + 256 * k, lane = e >> 3, j = e & 7, r = lane & 31, h = lane >> 5;
      unsigned short v;
      if (b < 18) {
        const int db = b / 6, ks = b % 6;
        v = wq[(16 * ks + 8 * h + j) * 96 + 32 * db + r];
        ((unsigned short*)(ws + WS_WQF))[b * 512 + e] = v;
      } else {
        const int bb = b - 18, ob = bb / 6, s = bb % 6;
        const int d = 32 * (s >> 1) + 16 * (s & 1) + 8 * (j >> 2) + 4 * h + (j & 3);
        v = wo[d * 96 + 32 * ob + r];
        ((unsigned short*)(ws + WS_WOF))[bb * 512 + e] = v;
      }
    }
  }
}

constexpr int RING = 4;
constexpr int LDS_W = RING * TILE_BYTES;
constexpr int LDS_MISC = LDS_W + 36 * 1024;
constexpr int LDS_CMB = LDS_MISC + 256;
constexpr int LDS_BYTES = LDS_CMB + 2 * 64 * 49 * 4;

DI void dma16(const char* gsrc, unsigned lds_dst) {
  unsigned keep;
  asm volatile("s_mov_b32 %0, m0\n\ts_mov_b32 m0, %2\n\ts_nop 0\n\tglobal_load_lds_dwordx4 %1, off\n\ts_mov_b32 m0, %0"
               : "=&s"(keep) : "v"(gsrc), "s"(lds_dst) : "memory");
}
DI bf16x8 pack8(const f32x16& s, int base) {
  u32x4 w;
  w[0] = cvtpk(s[base + 0], s[base + 1]); w[1] = cvtpk(s[base + 2], s[base + 3]);
  w[2] = cvtpk(s[base + 4], s[base + 5]); w[3] = cvtpk(s[base + 6], s[base + 7]);
  return __builtin_bit_cast(bf16x8, w);
}
DI float swap32_add(float v) {
  auto r = __builtin_amdgcn_permlane32_swap(__float_as_uint(v), __float_as_uint(v), false, false);
  return __uint_as_float(r[0]) + __uint_as_float(r[1]);
}
DI float swap32_max(float v) {
  auto r = __builtin_amdgcn_permlane32_swap(__float_as_uint(v), __float_as_uint(v), false, false);
  return fmaxf(__uint_as_float(r[0]), __uint_as_float(r[1]));
}
DI void mfma_v_init(f32x16& d, const bf16x8& a, const bf16x8& b, const f32x16& c) {
  asm volatile("v_mfma_f32_32x32x16_bf16 %0, %1, %2, %3" : "=&v"(d) : "v"(a), "v"(b), "v"(c));
}
DI void mfma_v_acc(f32x16& d, const bf16x8& a, const bf16x8& b) {
  asm volatile("v_mfma_f32_32x32x16_bf16 %0, %1, %2, %0" : "+v"(d) : "v"(a), "v"(b));
}
DI float fadd_s(float a, float b) { float r; asm("v_add_f32_e32 %0, %1, %2" : "=v"(r) : "v"(a), "v"(b)); return r; }
#define SB0() __builtin_amdgcn_sched_barrier(0)
#define LDSFRAG(base, i) (*reinterpret_cast<const bf16x8*>((base) + (i) * 1024))

template <int NQB> struct KState {
  f32x16 sA[NQB], sB[NQB];
  bf16x8 pkA[NQB][2], pkB[NQB][2];
  bf16x8 kfA[6], kfB[6], vfA[6], vfB[6];
  float l0[NQB], l1[NQB];
};

template <int NQB, int H, int G, int NG>
DI void sm_slice(f32x16 (&sx)[NQB], bf16x8 (&pk)[NQB][2], float (&l0)[NQB], float (&l1)[NQB]) {
  constexpr int NE = 8 * NQB;
  constexpr int NGE = NG - 1;
  constexpr int lo = (G < NGE) ? (G * NE) / NGE : NE, hi = (G < NGE) ? ((G + 1) * NE) / NGE : NE;
  constexpr int plo = (G >= 1) ? (((G - 1) < NGE) ? ((G - 1) * NE) / NGE : NE) : 0, phi = (G >= 1) ? lo : 0;
  if constexpr (H == 0 && G == 0) asm volatile("s_nop 7\n\ts_nop 3" : "+v"(sx[0]), "+v"(sx[NQB - 1]));
#pragma unroll
  for (int e = lo; e < hi; ++e) {
    const int ee = H * NE + e, qb = ee >> 4, r = ee & 15;
    sx[qb][r] = __builtin_amdgcn_exp2f(sx[qb][r]);
  }
#pragma unroll
  for (int e = plo; e < phi; ++e) {
    const int ee = H * NE + e, qb = ee >> 4, r = ee & 15;
    if (r & 1) l1[qb] = fadd_s(l1[qb], sx[qb][r]); else l0[qb] = fadd_s(l0[qb], sx[qb][r]);
    if (r & 1) {
      u32x4 w = __builtin_bit_cast(u32x4, pk[qb][r >> 3]);
      w[(r >> 1) & 3] = cvtpk(sx[qb][r - 1], sx[qb][r]);
      pk[qb][r >> 3] = __builtin_bit_cast(bf16x8, w);
    }
  }
}

template <int NQB, bool DMA, bool DOM, bool DOS, bool DOR, int G>
DI void qk_gap(f32x16 (&sx)[NQB], const bf16x8 (&kf)[6], const bf16x8 (&qf)[NQB][6], const f32x16 (&negm)[NQB],
               f32x16 (&so)[NQB], bf16x8 (&pko)[NQB][2], float (&l0)[NQB], float (&l1)[NQB],
               bf16x8 (&rd)[6], const char* rdbase, const char* g, unsigned l) {
  constexpr int NG = 6 * NQB, s = G / NQB, qb = G % NQB;
  if constexpr (DOM) {
    if constexpr (s == 0) mfma_v_init(sx[qb], kf[0], qf[qb][0], negm[qb]);
    else mfma_v_acc(sx[qb], kf[s], qf[qb][s]);
  }
  if constexpr (DOR && G < 6) rd[G] = LDSFRAG(rdbase, G);
  if constexpr (DMA && G >= NG - 3) dma16(g + (G - (NG - 3)) * 1024, l + (G - (NG - 3)) * 1024);
  if constexpr (DOS) sm_slice<NQB, 1, G, NG>(so, pko, l0, l1);
  SB0();
}
template <int NQB, bool DOM, bool DOS, bool DOR, int G>
DI void pv_gap(f32x16 (&o)[NQB][3], const bf16x8 (&vf)[6], const bf16x8 (&pk)[NQB][2],
               f32x16 (&sn)[NQB], bf16x8 (&pkn)[NQB][2], float (&l0)[NQB], float (&l1)[NQB],
               bf16x8 (&rd)[6], const char* rdbase) {
  constexpr int NG = 6 * NQB, kk = G / (3 * NQB), rem = G % (3 * NQB), db = rem / NQB, qb = rem % NQB;
  if constexpr (DOM) o[qb][db] = MFMA32(vf[kk * 3 + db], pk[qb][kk], o[qb][db]);
  if constexpr (DOR && G < 6) rd[G] = LDSFRAG(rdbase, G);
  if constexpr (DOS) sm_slice<NQB, 0, G, NG>(sn, pkn, l0, l1);
  SB0();
}
template <int NQB, bool DMA, bool DOM, bool DOS, bool DOR, int... Gs, typename... A> DI void qk_phase(std::integer_sequence<int, Gs...>, A&&... a) { (qk_gap<NQB, DMA, DOM, DOS, DOR, Gs>(a...), ...); }
template <int NQB, bool DOM, bool DOS, bool DOR, int... Gs, typename... A> DI void pv_phase(std::integer_sequence<int, Gs...>, A&&... a) { (pv_gap<NQB, DOM, DOS, DOR, Gs>(a...), ...); }

template <int NQB, int HALF = 0>
DI void key_pass(const char* kvf_lane, const unsigned ring_dma, const char* smem_lane,
                 const bf16x8 (&qf)[NQB ? NQB : 1][6], const f32x16 (&negm)[NQB ? NQB : 1], f32x16 (&o)[NQB ? NQB : 1][3], float (&lsum)[NQB ? NQB : 1], const int boff = 0) {
  constexpr int NQ = NQB ? NQB : 1;
  asm volatile("s_waitcnt vmcnt(6)\n\ts_waitcnt lgkmcnt(0)" ::: "memory");
  __builtin_amdgcn_s_barrier();
  if constexpr (NQB == 0) {
    int slot = 0;
    for (int t = 0; t < NTILE; ++t) {
      asm volatile("s_waitcnt vmcnt(3)" ::: "memory");
      __builtin_amdgcn_s_barrier();
      int tn = t + 3; if (tn >= NTILE) tn -= NTILE;
      const int sn = (slot + 3) & 3;
      const char* g = kvf_lane + (size_t)tn * TILE_BYTES;
      const unsigned l = ring_dma + (unsigned)sn * TILE_BYTES;
#pragma unroll
      for (int i = 0; i < 3; ++i) dma16(g + i * 1024, l + i * 1024);
      slot = (slot + 1) & 3;
    }
  } else {
    KState<NQ> k;
    using seq = std::make_integer_sequence<int, 6 * NQ>;
#pragma unroll
    for (int q = 0; q < NQ; ++q) {
      k.l0[q] = 0.f; k.l1[q] = 0.f;
#pragma unroll
      for (int r = 0; r < 16; ++r) { k.sB[q][r] = -1.0e30f; }
#pragma unroll
      for (int i = 0; i < 2; ++i) k.pkB[q][i] = bf16x8{0, 0, 0, 0, 0, 0, 0, 0};
    }
#pragma unroll
    for (int i = 0; i < 6; ++i) { k.vfB[i] = bf16x8{0, 0, 0, 0, 0, 0, 0, 0}; k.kfA[i] = LDSFRAG(smem_lane, i); }
    int slot = 0;
    for (int t = 0; t < NTILE; ++t) {
      const char* sl = smem_lane + slot * TILE_BYTES;
      const int nslot = (slot + 1) & 3;
      constexpr bool DA = (HALF != 2), DB = (HALF != 1);
      qk_phase<NQ, false, DA, DB, DB>(seq{}, k.sA, k.kfA, qf, negm, k.sB, k.pkB, k.l0, k.l1, k.kfB, sl + 6 * 1024 - boff, (const char*)nullptr, 0u);
      pv_phase<NQ, DB, DA, DA>(seq{}, o, k.vfB, k.pkB, k.sA, k.pkA, k.l0, k.l1, k.vfA, sl + 12 * 1024);
      asm volatile("s_waitcnt vmcnt(3)\n\ts_waitcnt lgkmcnt(0)" ::: "memory");
      __builtin_amdgcn_s_barrier();
      int tn = t + 3; if (tn >= NTILE) tn -= NTILE;
      const char* g = kvf_lane + (size_t)tn * TILE_BYTES;
      const unsigned l = ring_dma + (unsigned)((slot + 3) & 3) * TILE_BYTES;
      qk_phase<NQ, true, DB, DA, DA>(seq{}, k.sB, k.kfB, qf, negm, k.sA, k.pkA, k.l0, k.l1, k.kfA, smem_lane + nslot * TILE_BYTES, g, l);
      pv_phase<NQ, DA, DB, DB>(seq{}, o, k.vfA, k.pkA, k.sB, k.pkB, k.l0, k.l1, k.vfB, sl + 18 * 1024 - boff);
      slot = nslot;
    }
    if constexpr (HALF != 1) {
#pragma unroll
    for (int e = 0; e < 8 * NQ; ++e) {
      const int ee = 8 * NQ + e, qb = ee >> 4, r = ee & 15;
      k.sB[qb][r] = __builtin_amdgcn_exp2f(k.sB[qb][r]);
      if (r & 1) k.l1[qb] += k.sB[qb][r]; else k.l0[qb] += k.sB[qb][r];
    }
    if constexpr (NQ == 2) { k.pkB[1][0] = pack8(k.sB[1], 0); k.pkB[1][1] = pack8(k.sB[1], 8); }
    else k.pkB[0][1] = pack8(k.sB[0], 8);
#pragma unroll
    for (int kk = 0; kk < 2; ++kk)
#pragma unroll
      for (int db = 0; db < 3; ++db)
#pragma unroll
        for (int qb = 0; qb < NQ; ++qb) o[qb][db] = MFMA32(k.vfB[kk * 3 + db], k.pkB[qb][kk], o[qb][db]);
    }
#pragma unroll
    for (int q = 0; q < NQ; ++q) lsum[q] = k.l0[q] + k.l1[q];
  }
}

template <int NQB>
DI void max_pass(const char* kvf_lane, const unsigned ring_dma, const char* smem_lane,
                 const bf16x8 (&qf)[NQB ? NQB : 1][6], float (&rmax)[NQB ? NQB : 1]) {
  asm volatile("s_waitcnt vmcnt(6)\n\ts_waitcnt lgkmcnt(0)" ::: "memory");
  __builtin_amdgcn_s_barrier();
  int slot = 0;
  for (int t = 0; t < NTILE; ++t) {
    if constexpr (NQB > 0) {
      const char* sl = smem_lane + slot * TILE_BYTES;
#pragma unroll
      for (int kb = 0; kb < 2; ++kb)
#pragma unroll
        for (int q = 0; q < NQB; ++q) {
          f32x16 s;
#pragma unroll
          for (int r = 0; r < 16; ++r) s[r] = 0.f;
#pragma unroll
          for (int ss = 0; ss < 6; ++ss) s = MFMA32(LDSFRAG(sl, kb * 6 + ss), qf[q][ss], s);
#pragma unroll
          for (int r = 0; r < 16; ++r) rmax[q] = fmaxf(rmax[q], s[r]);
        }
    }
    asm volatile("s_waitcnt vmcnt(3)\n\ts_waitcnt lgkmcnt(0)" ::: "memory");
    __builtin_amdgcn_s_barrier();
    int tn = t + 3; if (tn >= NTILE) tn -= NTILE;
    const char* g = kvf_lane + (size_t)tn * TILE_BYTES;
    const unsigned l = ring_dma + (unsigned)((slot + 3) & 3) * TILE_BYTES;
#pragma unroll
    for (int i = 0; i < 3; ++i) dma16(g + i * 1024, l + i * 1024);
    slot = (slot + 1) & 3;
  }
}

template <int NQB, int HALF = 0>
DI void do_round(const int row0, bf16x8 (&xb)[6], const unsigned short* __restrict__ xnext, unsigned short* __restrict__ out, char* smem,
                 const char* kvf_lane, const unsigned ring_dma, const int lane, const int tid, const int wid, const float kmax, const float shift, const bool split = false) {
  constexpr int NQ = NQB ? NQB : 1;
  const int r32 = lane & 31, hi = lane >> 5;
  const char* smem_lane = smem + lane * 16;
  float* misc = (float*)(smem + LDS_MISC);
  bf16x8 qf[NQ][6]; f32x16 negm[NQ]; f32x16 o[NQ][3]; float lsum[NQ];
  if constexpr (NQB > 0) {
    const char* wqf = smem + LDS_W + lane * 16;
#pragma unroll
    for (int q = 0; q < NQ; ++q) {
      f32x16 qa[3];
#pragma unroll
      for (int db = 0; db < 3; ++db)
#pragma unroll
        for (int r = 0; r < 16; ++r) qa[db][r] = 0.f;
#pragma unroll
      for (int ks = 0; ks < 6; ++ks) {
#pragma unroll
        for (int db = 0; db < 3; ++db) qa[db] = MFMA32(LDSFRAG(wqf, db * 6 + ks), xb[ks], qa[db]);
      }
      if (xnext) {
#pragma unroll
        for (int ks = 0; ks < 6; ++ks) xb[ks] = *reinterpret_cast<const bf16x8*>(xnext + ks * 16);
      }
      float qn = 0.f;
#pragma unroll
      for (int s = 0; s < 6; ++s) {
        qf[q][s] = pack8(qa[s >> 1], 8 * (s & 1));
        const u32x4 w = __builtin_bit_cast(u32x4, qf[q][s]);
#pragma unroll
        for (int e = 0; e < 4; ++e) {
          const float a = __uint_as_float(w[e] << 16), bb = __uint_as_float(w[e] & 0xffff0000u);
          qn = fmaf(a, a, qn); qn = fmaf(bb, bb, qn);
        }
      }
      qn = swap32_add(qn);
      const float mb = sqrtf(qn) * kmax * 1.001f + 0.01f - shift;
#pragma unroll
      for (int r = 0; r < 16; ++r) negm[q][r] = -mb;
#pragma unroll
      for (int db = 0; db < 3; ++db)
#pragma unroll
        for (int r = 0; r < 16; ++r) o[q][db][r] = 0.f;
    }
  }
  key_pass<NQB, (HALF ? 2 : 0)>(kvf_lane, ring_dma, smem_lane, qf, negm, o, lsum, HALF == 1 ? 6144 : 0);
  if constexpr (HALF != 0) {
    float* cmb = (float*)(smem + LDS_CMB) + (wid & 1) * 64 * 49;
    if constexpr (HALF == 2) {
#pragma unroll
      for (int db = 0; db < 3; ++db)
#pragma unroll
        for (int r = 0; r < 16; ++r) cmb[(db * 16 + r) * 64 + lane] = o[0][db][r];
      cmb[48 * 64 + lane] = lsum[0];
    }
    __syncthreads();
    if constexpr (HALF == 1) {
#pragma unroll
      for (int db = 0; db < 3; ++db)
#pragma unroll
        for (int r = 0; r < 16; ++r) o[0][db][r] += cmb[(db * 16 + r) * 64 + lane];
      lsum[0] += cmb[48 * 64 + lane];
    }
    __syncthreads();
  } else if constexpr (NQB == 0) { if (split) { __syncthreads(); __syncthreads(); } }
  float ltot[NQ];
  bool bad = false;
  if constexpr (NQB > 0) {
#pragma unroll
    for (int q = 0; q < NQ; ++q) { ltot[q] = swap32_add(lsum[q]); bad = bad || (HALF != 2 && !(ltot[q] >= 8.673617379884035e-19f)); }
  }
  {
    const bool wbad = __any(bad);
    if (lane == 0) misc[16 + wid] = wbad ? 1.f : 0.f;
  }
  __syncthreads();
  float flag = 0.f;
#pragma unroll
  for (int i = 0; i < 8; ++i) flag += misc[16 + i];
  if (flag != 0.f) {
    float rmax[NQ];
#pragma unroll
    for (int q = 0; q < NQ; ++q) rmax[q] = -3.0e38f;
    max_pass<NQB>(kvf_lane, ring_dma, smem_lane, qf, rmax);
    if constexpr (NQB > 0) {
#pragma unroll
      for (int q = 0; q < NQ; ++q) {
        const float m = swap32_max(rmax[q]);
#pragma unroll
        for (int r = 0; r < 16; ++r) negm[q][r] = -m;
#pragma unroll
        for (int db = 0; db < 3; ++db)
#pragma unroll
          for (int r = 0; r < 16; ++r) o[q][db][r] = 0.f;
      }
    }
    key_pass<NQB, (HALF ? 2 : 0)>(kvf_lane, ring_dma, smem_lane, qf, negm, o, lsum, HALF == 1 ? 6144 : 0);
  if constexpr (HALF != 0) {
    float* cmb = (float*)(smem + LDS_CMB) + (wid & 1) * 64 * 49;
    if constexpr (HALF == 2) {
#pragma unroll
      for (int db = 0; db < 3; ++db)
#pragma unroll
        for (int r = 0; r < 16; ++r) cmb[(db * 16 + r) * 64 + lane] = o[0][db][r];
      cmb[48 * 64 + lane] = lsum[0];
    }
    __syncthreads();
    if constexpr (HALF == 1) {
#pragma unroll
      for (int db = 0; db < 3; ++db)
#pragma unroll
        for (int r = 0; r < 16; ++r) o[0][db][r] += cmb[(db * 16 + r) * 64 + lane];
      lsum[0] += cmb[48 * 64 + lane];
    }
    __syncthreads();
  } else if constexpr (NQB == 0) { if (split) { __syncthreads(); __syncthreads(); } }
    if constexpr (NQB > 0) {
#pragma unroll
      for (int q = 0; q < NQ; ++q) ltot[q] = swap32_add(lsum[q]);
    }
  }
  if constexpr (NQB > 0 && HALF != 2) {
    const char* wof = smem + LDS_W + 18 * 1024 + lane * 16;
#pragma unroll
    for (int q = 0; q < NQ; ++q) {
      const float inv = 1.0f / ltot[q];
      bf16x8 on[6];
#pragma unroll
      for (int db = 0; db < 3; ++db)
#pragma unroll
        for (int r = 0; r < 16; ++r) o[q][db][r] *= inv;
#pragma unroll
      for (int s = 0; s < 6; ++s) on[s] = pack8(o[q][s >> 1], 8 * (s & 1));
      unsigned short* orow = out + (size_t)(row0 + q * 32 + r32) * CH + 4 * hi;
#pragma unroll
      for (int ob = 0; ob < 3; ++ob) {
        f32x16 acc;
#pragma unroll
        for (int r = 0; r < 16; ++r) acc[r] = 0.f;
#pragma unroll
        for (int s = 0; s < 6; ++s) acc = MFMA32(LDSFRAG(wof, ob * 6 + s), on[s], acc);
#pragma unroll
        for (int g = 0; g < 4; ++g) {
          u32x2 w;
          w[0] = cvtpk(acc[4 * g], acc[4 * g + 1]); w[1] = cvtpk(acc[4 * g + 2], acc[4 * g + 3]);
          *reinterpret_cast<u32x2*>(orow + 32 * ob + 8 * g) = w;
        }
      }
    }
  }
}

__global__ __launch_bounds__(512, 2) void attn_kernel(const unsigned short* __restrict__ x, const char* __restrict__ ws,
                                                      unsigned short* __restrict__ out) {
  extern __shared__ __attribute__((aligned(16))) char smem[];
  const int tid = threadIdx.x, lane = tid & 63;
  const int wid = __builtin_amdgcn_readfirstlane(tid >> 6);
  const int wg = blockIdx.x;
  const char* kvf_lane = ws + WS_KVF + wid * 3072 + lane * 16;
  const unsigned ring_dma = (unsigned)(uintptr_t)smem + (unsigned)wid * 3072u;
  float* misc = (float*)(smem + LDS_MISC);
  (void)misc;
#pragma unroll
  for (int t = 0; t < 3; ++t)
#pragma unroll
    for (int i = 0; i < 3; ++i) dma16(kvf_lane + t * TILE_BYTES + i * 1024, ring_dma + t * TILE_BYTES + i * 1024);
  for (int p = wid; p < 36; p += 8)
    dma16(ws + WS_WQF + p * 1024 + lane * 16, (unsigned)(uintptr_t)smem + LDS_W + p * 1024);
  bf16x8 xb[6];
  {
    const unsigned short* xr = x + (size_t)(wg * 512 + wid * 32 + (lane & 31)) * CH + (lane >> 5) * 8;
#pragma unroll
    for (int ks = 0; ks < 6; ++ks) xb[ks] = *reinterpret_cast<const bf16x8*>(xr + ks * 16);
  }
  float kn = 0.f, va = 0.f;
  {
    const float* kn2 = (const float*)(ws + WS_KN2);
    const float* vam = (const float*)(ws + WS_VAM);
    for (int i = tid; i < NKEY; i += 512) { kn = fmaxf(kn, kn2[i]); const float v = vam[i]; va = (v > va || v != v) ? v : va; }
#pragma unroll
    for (int off = 32; off >= 1; off >>= 1) {
      kn = fmaxf(kn, __shfl_xor(kn, off));
      const float v = __shfl_xor(va, off); va = (v > va || v != v) ? v : va;
    }
    if (lane == 0) { misc[wid] = kn; misc[8 + wid] = va; }
    __syncthreads();
    kn = misc[0]; va = misc[8];
#pragma unroll
    for (int i = 1; i < 8; ++i) { kn = fmaxf(kn, misc[i]); const float v = misc[8 + i]; va = (v > va || v != v) ? v : va; }
    __syncthreads();
  }
  const float kmax = sqrtf(kn) * 1.001f + 1e-6f;
  float shift;
  {
    const float lv = (va > 0.f) ? __log2f(va) : -200.f;
    float sh = floorf(110.f - lv);
    if (!(sh < 64.f)) sh = 64.f;
    if (!(sh > 0.f)) sh = 0.f;
    shift = sh;
  }
  for (int round = 0; round < 2; ++round) {
    const unsigned short* xn = (round == 0) ? x + (size_t)(wg * 512 + 256 + wid * 32 + (lane & 31)) * CH + (lane >> 5) * 8
                             : ((wid < 4) ? x + (size_t)(131072 + wg * 64 + (wid & 1) * 32 + (lane & 31)) * CH + (lane >> 5) * 8 : nullptr);
    do_round<1>(wg * 512 + round * 256 + wid * 32, xb, xn, out, smem, kvf_lane, ring_dma, lane, tid, wid, kmax, shift);
  }
  if (wid < 2)      do_round<1, 1>(131072 + wg * 64 + (wid & 1) * 32, xb, nullptr, out, smem, kvf_lane, ring_dma, lane, tid, wid, kmax, shift);
  else if (wid < 4) do_round<1, 2>(131072 + wg * 64 + (wid & 1) * 32, xb, nullptr, out, smem, kvf_lane, ring_dma, lane, tid, wid, kmax, shift);
  else              do_round<0>(0, xb, nullptr, out, smem, kvf_lane, ring_dma, lane, tid, wid, kmax, shift, true);
  asm volatile("s_waitcnt vmcnt(0)" ::: "memory");
}

extern "C" void kernel_launch(void* const* d_in, const int* in_sizes, int n_in, void* d_out, int out_size, void* d_ws,
                              size_t ws_size, hipStream_t stream) {
  const unsigned short* x = (const unsigned short*)d_in[0];
  const unsigned short* wq = (const unsigned short*)d_in[1];
  const unsigned short* wkv = (const unsigned short*)d_in[2];
  const unsigned short* wo = (const unsigned short*)d_in[3];
  hipFuncSetAttribute((const void*)attn_kernel, hipFuncAttributeMaxDynamicSharedMemorySize, LDS_BYTES);
  hipLaunchKernelGGL(prep_kernel, dim3(288), dim3(256), 0, stream, x, wq, wkv, wo, (char*)d_ws);
  hipLaunchKernelGGL(attn_kernel, dim3(256), dim3(512), LDS_BYTES, stream, x, (const char*)d_ws, (unsigned short*)d_out);
}
```
